```python
import torch

def _softmax(x, axis=-1):
    return torch.softmax(x, dim=axis)
def _gelu(x):
    return torch.nn.functional.gelu(x, approximate="tanh")
def _silu(x):
    return torch.nn.functional.silu(x)
def _sigmoid(x):
    return torch.sigmoid(x)
def _layernorm(x, axis=-1, eps=1e-5):
    m = x.mean(dim=axis, keepdim=True)
    v = x.var(dim=axis, keepdim=True, unbiased=False)
    return (x - m) / torch.sqrt(v + eps)
def _scatter_add(tgt, idx, src, axis=0):
    return tgt.clone().index_add_(axis, idx, src)
def _segment_sum(src, seg, n):
    out = torch.zeros((n,) + src.shape[1:], dtype=src.dtype)
    return out.index_add_(0, seg, src)
def _pad(x, pw):
    flat = tuple(v for pair in reversed(pw) for v in pair)
    return torch.nn.functional.pad(x, flat)

kernel_name = 'gated_mix__mha_block__dense_activation_mish__prompt'

def setup_inputs():

    _w = (lambda a: (lambda t: t.to(torch.bfloat16) if t.is_floating_point() else t)(torch.as_tensor(a)))
    def _A_setup():
        B, S, D, H = 1, 2048, 512, 32
        return {
            "x": _w((lambda *s: torch.randn(*s, dtype=torch.bfloat16) * 0.05)(B, S, D)),
            "wqkv": _w((lambda *s: torch.randn(*s, dtype=torch.bfloat16) * 0.8)(D, 3 * D)),
            "wo": _w((lambda *s: torch.randn(*s, dtype=torch.bfloat16) * 0.05)(D, D)),
        }
    _A_in = _A_setup()

    def _B_setup():
        B, S, D, F = 1, 2048, 512, 1376
        return {
            "x": _w((lambda *s: torch.randn(*s, dtype=torch.bfloat16) * 0.05)(B, S, D)),
            "w1": _w((lambda *s: torch.randn(*s, dtype=torch.bfloat16) * 12.8)(D, F) * (D ** -0.5)),
            "b1": _w((lambda *s: torch.randn(*s, dtype=torch.bfloat16) * 0.2)(F)),
            "w2": _w((lambda *s: torch.randn(*s, dtype=torch.bfloat16) * 0.1)(F, D) * (F ** -0.5)),
        }
    _B_in = _B_setup()

    return {
        "A_x": _A_in["x"],
        "A_wqkv": _A_in["wqkv"],
        "A_wo": _A_in["wo"],
        "B_x": _B_in["x"],
        "B_w1": _B_in["w1"],
        "B_b1": _B_in["b1"],
        "B_w2": _B_in["w2"],
        "gate_w": _w((lambda *s: torch.randn(*s, dtype=torch.bfloat16) * 0.05)(512) * 16.0),
    }

def reference(A_x, A_wqkv, A_wo, B_x, B_w1, B_b1, B_w2, gate_w):
    def _A_ref(x, wqkv, wo):
        B, S, D = x.shape
        H = 32
        Dh = D // H
        qkv = (lambda x, *d: x.permute(*d))((x @ wqkv).reshape(B, S, 3, H, Dh), 2, 0, 3, 1, 4)
        q, k, v = qkv[0], qkv[1], qkv[2]
        scores = torch.einsum("bhqd,bhkd->bhqk", q, k) * (Dh ** -0.5)
        p = _softmax(scores, axis=-1)
        o = torch.einsum("bhqk,bhkd->bhqd", p, v)
        return (lambda x, *d: x.permute(*d))(o, 0, 2, 1, 3).reshape(B, S, D) @ wo

    def _B_ref(x, w1, b1, w2):
        h = torch.einsum("bsd,df->bsf", x, w1) + b1
        sp = torch.log1p(torch.exp(-torch.abs(h))) + torch.where(h > 0, h, torch.zeros_like(h))
        act = h * torch.tanh(sp)
        return torch.einsum("bsf,fd->bsd", act, w2)

    x = A_x
    g = _sigmoid((x * gate_w).sum(-1, keepdims=True))
    a = _A_ref(x, A_wqkv, A_wo)
    b = _B_ref(x, B_w1, B_b1, B_w2)
    return g * a + (1 - g) * b
```

```cpp
#include <hip/hip_runtime.h>
#include <hip/hip_bf16.h>
#include <stdint.h>

typedef __attribute__((ext_vector_type(8))) short bf16x8;
typedef __attribute__((ext_vector_type(4))) short s16x4;
typedef __attribute__((ext_vector_type(16))) float f32x16;
typedef __attribute__((ext_vector_type(4))) float f32x4;
typedef __attribute__((ext_vector_type(2))) float f32x2;
typedef __attribute__((ext_vector_type(4))) unsigned u32x4;
typedef __attribute__((ext_vector_type(2))) unsigned u32x2;
typedef __attribute__((ext_vector_type(2))) __bf16 bf16x2_t;
typedef unsigned short u16;
typedef __attribute__((address_space(3))) const char* lds_cptr;
typedef __attribute__((address_space(3))) char* lds_ptr;
typedef short v4i16_t __attribute__((ext_vector_type(4)));

#define DI __device__ __forceinline__
#define MFMA(a, b, c) __builtin_amdgcn_mfma_f32_32x32x16_bf16((a), (b), (c), 0, 0, 0)

constexpr int SEQ = 2048, DM = 512, NH = 32, FF = 1376;
constexpr int NUF = FF / 32;

constexpr size_t WS_Q = 0;
constexpr size_t WS_K = WS_Q + 2u * 1024 * 1024;
constexpr size_t WS_V = WS_K + 2u * 1024 * 1024;
constexpr size_t WS_O = WS_V + 2u * 1024 * 1024;
constexpr size_t WS_B = WS_O + 2u * 1024 * 1024;
constexpr size_t WS_H = WS_B + 2u * 1024 * 1024;
constexpr size_t WS_W2 = WS_H + (size_t)SEQ * FF * 2;
constexpr size_t WS_WO = WS_W2 + (size_t)FF * DM * 2;
constexpr size_t WS_KM = WS_WO + (size_t)DM * DM * 2;
constexpr size_t WS_G = WS_KM + 32 * 64 * 4;

DI unsigned cvtpk(float lo, float hi) {
  f32x2 v = {lo, hi};
  bf16x2_t b = __builtin_convertvector(v, bf16x2_t);
  return __builtin_bit_cast(unsigned, b);
}
DI float bf2f(u16 v) { return __uint_as_float((unsigned)v << 16); }
DI float bfr(float x) { return __uint_as_float(cvtpk(x, x) & 0xffff0000u); }
DI float lo2f(unsigned v) { return __uint_as_float(v << 16); }
DI float hi2f(unsigned v) { return __uint_as_float(v & 0xffff0000u); }

DI void glds16(const void* sbase, unsigned voff, unsigned lds_base) {
  unsigned sv;
  asm volatile("s_mov_b32 %0, m0\n\ts_mov_b32 m0, %3\n\ts_nop 0\n\tglobal_load_lds_dwordx4 %1, %2\n\ts_mov_b32 m0, %0"
               : "=&s"(sv)
               : "v"(voff), "s"(sbase), "s"(lds_base)
               : "memory");
}
#define WAITV_BAR(N) asm volatile("s_waitcnt vmcnt(" #N ") lgkmcnt(0)\n\ts_barrier" ::: "memory")

DI void st16(void* p, u32x4 v) {
  asm volatile("global_store_dwordx4 %0, %1, off sc1\n\ts_nop 1" ::"v"(p), "v"(v) : "memory");
}
DI void st16(void* p, bf16x8 v) { st16(p, __builtin_bit_cast(u32x4, v)); }
DI bf16x8 lds_b128(lds_cptr p) { return *(const __attribute__((address_space(3))) bf16x8*)p; }
DI s16x4 lds_tr(lds_cptr p) {
  return __builtin_bit_cast(s16x4, __builtin_amdgcn_ds_read_tr16_b64_v4i16((__attribute__((address_space(3))) v4i16_t*)p));
}
DI bf16x8 pack8(const f32x16& a, int o) {
  u32x4 p = {cvtpk(a[o], a[o + 1]), cvtpk(a[o + 2], a[o + 3]), cvtpk(a[o + 4], a[o + 5]), cvtpk(a[o + 6], a[o + 7])};
  return __builtin_bit_cast(bf16x8, p);
}
DI float swap32_add(float v) {
  auto r = __builtin_amdgcn_permlane32_swap(__float_as_uint(v), __float_as_uint(v), false, false);
  return __uint_as_float(r[0]) + __uint_as_float(r[1]);
}
DI float swap32_max(float v) {
  auto r = __builtin_amdgcn_permlane32_swap(__float_as_uint(v), __float_as_uint(v), false, false);
  return fmaxf(__uint_as_float(r[0]), __uint_as_float(r[1]));
}

#define K1_SL(s) (((s) % 5) * 32768)
DI void glds16_4(const char* b0, unsigned v0, const char* b1, unsigned v1, const char* b2, unsigned v2, const char* b3, unsigned v3,
                 unsigned lds_base) {
  unsigned sv;
  asm volatile("s_mov_b32 %0, m0\n\ts_mov_b32 m0, %9\n\ts_nop 0\n\tglobal_load_lds_dwordx4 %1, %2\n\t"
               "global_load_lds_dwordx4 %3, %4 offset:1024\n\tglobal_load_lds_dwordx4 %5, %6 offset:2048\n\t"
               "global_load_lds_dwordx4 %7, %8 offset:3072\n\ts_mov_b32 m0, %0"
               : "=&s"(sv)
               : "v"(v0), "s"(b0), "v"(v1), "s"(b1 - 1024), "v"(v2), "s"(b2 - 2048), "v"(v3), "s"(b3 - 3072), "s"(lds_base)
               : "memory");
}
constexpr float QSCALE = 0.25f * 1.4426950408889634f;
#define SB() __builtin_amdgcn_sched_barrier(0)
#define BAR() asm volatile("s_barrier" ::: "memory")

DI float mish_f(float h) {
  float hc = __builtin_amdgcn_fmed3f(h, -100.f, 20.f);
  float n = __builtin_amdgcn_exp2f(hc * 1.4426950408889634f);
  float t = n * (n + 2.f);
  return h * t * __builtin_amdgcn_rcpf(t + 2.f);
}
DI bf16x8 wfrag(lds_cptr p) {
  s16x4 lo = lds_tr(p), hi = lds_tr(p + 256);
  return __builtin_shufflevector(lo, hi, 0, 1, 2, 3, 4, 5, 6, 7);
}

template <int TYPE>
DI void k1_body(const u16* __restrict__ x, const u16* __restrict__ wsrc, const int ld, const int n0,
                const int c, const u16* __restrict__ wo, const u16* __restrict__ b1, const u16* __restrict__ w2,
                const u16* __restrict__ gate_w, char* __restrict__ ws, char* lds, const int w, const int lane,
                const int mt, const int j, const int b) {
  const int m0 = mt * 128, wr = w & 3, wc = w >> 2, r = lane & 31, h = lane >> 5;
  const unsigned lds0 = (unsigned)(uintptr_t)lds;
  lds_cptr L = (lds_cptr)lds;
  unsigned xvo[2], wvo[3];
#pragma unroll
  for (int t = 0; t < 2; ++t) {
    int i = 2 * w + t, row = 8 * i + (lane >> 3), p = lane & 7, cc = p ^ ((row >> 1) & 7);
    xvo[t] = ((m0 + row) * DM + 8 * cc) * 2;
  }
#pragma unroll
  for (int u = 0; u < 3; ++u) {
    int col = min(n0 + 32 * u + 8 * (lane & 3), ld - 8);
    wvo[u] = ((16 * wr + (lane >> 2)) * ld + col) * 2;
  }
  const char* xb = (const char*)x;
  const char* wbs = (const char*)wsrc;
  const size_t wst = (size_t)64 * ld * 2;
  const int rot = (mt + j) & 7;
#define KC(s) (((s) + rot) & 7)
#define ISSUE_ST(s) glds16_4(xb + KC(s) * 128, xvo[0], xb + KC(s) * 128, xvo[1], wbs + KC(s) * wst, wvo[0], wbs + KC(s) * wst, wvo[1], lds0 + K1_SL(s) + w * 4096)
#define ISSUE_C4(i) glds16_4(wbs + KC(4 * (i)) * wst, wvo[2], wbs + KC(4 * (i) + 1) * wst, wvo[2], wbs + KC(4 * (i) + 2) * wst, wvo[2], wbs + KC(4 * (i) + 3) * wst, wvo[2], lds0 + K1_SL(3 + (i)) + w * 4096)
  u32x2 bias[3][4];
  if (TYPE == 2) {
#pragma unroll
    for (int u = 0; u < 3; ++u) {
      const u16* bp = b1 + min(32 * (3 * (c - 17) + u), FF - 32) + 4 * h;
      asm volatile("global_load_dwordx2 %0, %4, off\n\tglobal_load_dwordx2 %1, %4, off offset:16\n\t"
                   "global_load_dwordx2 %2, %4, off offset:32\n\tglobal_load_dwordx2 %3, %4, off offset:48"
                   : "=&v"(bias[u][0]), "=&v"(bias[u][1]), "=&v"(bias[u][2]), "=&v"(bias[u][3]) : "v"(bp) : "memory");
    }
  }
  ISSUE_ST(0); BAR(); ISSUE_ST(1);
  BAR(); ISSUE_ST(2); BAR(); ISSUE_ST(3);

  const unsigned xo0 = lds0 + (2 * wr + (r >> 4)) * 4096 + ((r >> 3) & 1) * 1024 + (r & 7) * 128 + ((h ^ ((r >> 1) & 7)) * 16);
  const unsigned wlane = (8 * h + ((lane & 15) >> 2)) * 64 + ((lane >> 4) & 1) * 32 + (lane & 3) * 8;
  lds_cptr wa0 = L + wc * 16384 + wlane, wa1 = wa0 + 65536, wa2 = wa0 + 131072;
  asm volatile("" : "+v"(wa1), "+v"(wa2));
#define SLADDR(sl) ((sl) < 2 ? wa0 + (sl) * 32768 : (sl) < 4 ? wa1 + ((sl) - 2) * 32768 : wa2)
#define W01ADDR(s, u, k) (SLADDR((s) % 5) + (k) * 4096 + 2048 + (u) * 1024)
#define W2ADDR(g) (SLADDR(3 + ((g) >> 4)) + ((g) & 3) * 4096 + (((g) >> 2) & 3) * 1024)
  const int tt = mt * 4 + wr;
  bf16x8 xf[32];
  f32x16 zero;
#pragma unroll
  for (int i = 0; i < 16; ++i) zero[i] = 0.f;
  f32x16 acc0, acc1;

  auto epilogue = [&](int u, const f32x16& acc) {
    if (TYPE == 0) {
      const int unit = 3 * c + u;
      if (unit < 16) {
#pragma unroll
        for (int hh = 0; hh < 2; ++hh) {
          const int head = 2 * unit + hh;
          u32x4 o;
#pragma unroll
          for (int i = 0; i < 4; ++i) o[i] = cvtpk(acc[8 * hh + 2 * i] * QSCALE, acc[8 * hh + 2 * i + 1] * QSCALE);
          st16(ws + WS_Q + ((size_t)(head * SEQ + tt * 32 + r) * 2 + h) * 16, o);
        }
      } else if (unit < 32) {
        float ss[2];
#pragma unroll
        for (int hh = 0; hh < 2; ++hh) {
          const int head = 2 * (unit - 16) + hh;
          u32x4 o;
          float s2 = 0.f;
#pragma unroll
          for (int i = 0; i < 4; ++i) {
            o[i] = cvtpk(acc[8 * hh + 2 * i], acc[8 * hh + 2 * i + 1]);
            float lo = lo2f(o[i]), hi = hi2f(o[i]);
            s2 += lo * lo + hi * hi;
          }
          st16(ws + WS_K + ((size_t)(head * 64 + tt) * 64 + lane) * 16, o);
          ss[hh] = swap32_add(s2);
        }
        float sv = h ? ss[1] : ss[0];
#pragma unroll
        for (int of = 16; of >= 1; of >>= 1) sv = fmaxf(sv, __shfl_xor(sv, of));
        if (r == 0) ((float*)(ws + WS_KM))[(2 * (unit - 16) + h) * 64 + tt] = sv;
      }
    } else if (TYPE == 1) {
      const int unit = 3 * (c - 11) + u;
      if (unit < 16) {
        const int head = 2 * unit + (r >> 4), d = r & 15;
#pragma unroll
        for (int s = 0; s < 2; ++s) st16(ws + WS_V + ((size_t)(head * 64 + tt) * 64 + s * 32 + h * 16 + d) * 16, pack8(acc, 8 * s));
      }
    } else {
      const int uf = 3 * (c - 17) + u;
      if (uf < NUF) {
#pragma unroll
        for (int s = 0; s < 2; ++s) {
          f32x16 a;
#pragma unroll
          for (int i = 0; i < 8; ++i) a[i] = mish_f(acc[8 * s + i]);
          st16(ws + WS_H + ((size_t)((tt * NUF + uf) * 2 + s) * 64 + lane) * 16, pack8(a, 0));
          SB();
        }
      }
    }
  };
#define MM(XF, WF, C) ((TYPE == 1) ? MFMA(XF, WF, C) : MFMA(WF, XF, C))
  bf16x8 wa[3], wbq[3];
  const unsigned xk[4] = {0u, 32u, 64u, 96u};
#define RDG(g) do { unsigned xa_; asm volatile("v_xad_u32 %0, %1, %2, %3" : "=v"(xa_) : "v"(xo0), "v"(xk[(g) & 3]), "s"((unsigned)(K1_SL((g) >> 2) & ~65535))); \
    xf[g] = lds_b128((lds_cptr)(uintptr_t)xa_ + (K1_SL((g) >> 2) & 65535)); wa[(g) % 3] = wfrag(W01ADDR((g) >> 2, 0, (g) & 3)); wbq[(g) % 3] = wfrag(W01ADDR((g) >> 2, 1, (g) & 3)); } while (0)
#define MMG(g) do { acc0 = MM(xf[g], wa[(g) % 3], ((g) == 0 && TYPE != 2) ? zero : acc0); acc1 = MM(xf[g], wbq[(g) % 3], ((g) == 0 && TYPE != 2) ? zero : acc1); } while (0)
#define BIAS_INIT(A, u) do { _Pragma("unroll") for (int q = 0; q < 4; ++q) { A[4 * q] = lo2f(bias[u][q][0]); A[4 * q + 1] = hi2f(bias[u][q][0]); A[4 * q + 2] = lo2f(bias[u][q][1]); A[4 * q + 3] = hi2f(bias[u][q][1]); } } while (0)
#define P1_STEP(s, N, ISS)              \
  do {                                                                             \
    if (wc == 0) { ISS; } SB();                                                    \
    RDG(4 * (s) + 2); SB(); MMG(4 * (s)); SB();                                    \
    RDG(4 * (s) + 3); SB(); MMG(4 * (s) + 1); SB();                                \
    if (wc != 0) { ISS; } SB();                                                    \
    if ((s) < 7) { WAITV_BAR(N); RDG(4 * (s) + 4); } SB(); MMG(4 * (s) + 2); SB(); \
    if ((s) < 7) { RDG(4 * (s) + 5); } SB(); MMG(4 * (s) + 3); SB();               \
  } while (0)
#define NOP_() do {} while (0)
  WAITV_BAR(12);
  RDG(0); RDG(1); SB();
  if (TYPE == 2) {
#pragma unroll
    for (int u = 0; u < 3; ++u)
      asm volatile("" : "+v"(bias[u][0]), "+v"(bias[u][1]), "+v"(bias[u][2]), "+v"(bias[u][3]));
    BIAS_INIT(acc0, 0); BIAS_INIT(acc1, 1);
  }
  SB();
  P1_STEP(0, 12, ISSUE_ST(4));
  P1_STEP(1, 12, ISSUE_ST(5));
  P1_STEP(2, 12, ISSUE_ST(6));
  P1_STEP(3, 12, ISSUE_ST(7));
  P1_STEP(4, 12, ISSUE_C4(0));
  P1_STEP(5, 12, ISSUE_C4(1));
  P1_STEP(6, 8, NOP_());
  P1_STEP(7, 0, NOP_());
#undef RDG
#undef MMG
#undef P1_STEP
  WAITV_BAR(0);

  SB();
  epilogue(0, acc0);
  SB();
  bf16x8 wq[4];
#pragma unroll
  for (int i = 0; i < 4; ++i) wq[i] = wfrag(W2ADDR(i));
  SB();
  epilogue(1, acc1);
  SB();

  unsigned t0, t1, t2, t3, t4, t5, t6, t7, t8, t9, t10, t11, t12, t13, t14, t15;
  u32x4 gxv, ggv, gxv2, ggv2;
  const int id = w * 256 + b;
  const bool tj = (w < 4) && (id < 688 + 256);
  const u16* tsrc; char* tdst; int tkb = 0, tnb = 0, tnks = 0;
  if (id < 688) { tsrc = w2; tdst = ws + WS_W2; tkb = id >> 4; tnb = id & 15; tnks = 86; }
  else { int t = id - 688; tsrc = wo; tdst = ws + WS_WO; tkb = t >> 4; tnb = t & 15; tnks = 32; }
  const int grow = m0 + 8 * j + 2 * wr;
  if (tj) {
    const u16* p = tsrc + (size_t)(32 * tkb + 4 * h) * DM + 32 * tnb + r;
#define LD4(D0, D1, D2, D3, P) asm volatile("global_load_ushort %0, %4, off\n\tglobal_load_ushort %1, %4, off offset:1024\n\t" \
                 "global_load_ushort %2, %4, off offset:2048\n\tglobal_load_ushort %3, %4, off offset:3072"       \
                 : "=&v"(D0), "=&v"(D1), "=&v"(D2), "=&v"(D3) : "v"(P) : "memory")
    LD4(t0, t1, t2, t3, p); LD4(t4, t5, t6, t7, p + 8 * DM); LD4(t8, t9, t10, t11, p + 16 * DM); LD4(t12, t13, t14, t15, p + 24 * DM);
#undef LD4
  }
  if (w < 4) {
    const u16* xp = x + (grow + h) * DM + r * 16;
    const u16* gp = gate_w + r * 16;
    asm volatile("global_load_dwordx4 %0, %4, off\n\tglobal_load_dwordx4 %1, %4, off offset:16\n\t"
                 "global_load_dwordx4 %2, %5, off\n\tglobal_load_dwordx4 %3, %5, off offset:16"
                 : "=&v"(gxv), "=&v"(gxv2), "=&v"(ggv), "=&v"(ggv2) : "v"(xp), "v"(gp) : "memory");
  }
  if (TYPE == 2) BIAS_INIT(acc0, 2);
  SB();
#pragma unroll
  for (int g = 0; g < 32; ++g) {
    acc0 = MM(xf[g], wq[g & 3], (g == 0 && TYPE != 2) ? zero : acc0);
    if (g + 4 < 32) wq[g & 3] = wfrag(W2ADDR(g + 4));
    SB();
  }
  if (w < 4) {
    asm volatile("s_waitcnt vmcnt(0)" : "+v"(t0), "+v"(t1), "+v"(t2), "+v"(t3), "+v"(t4), "+v"(t5), "+v"(t6), "+v"(t7),
                 "+v"(t8), "+v"(t9), "+v"(t10), "+v"(t11), "+v"(t12), "+v"(t13), "+v"(t14), "+v"(t15), "+v"(gxv), "+v"(gxv2), "+v"(ggv), "+v"(ggv2)::"memory");
    if (tj) {
      st16(tdst + ((size_t)(tnb * tnks + 2 * tkb) * 64 + lane) * 16, (u32x4){t0 | (t1 << 16), t2 | (t3 << 16), t4 | (t5 << 16), t6 | (t7 << 16)});
      st16(tdst + ((size_t)(tnb * tnks + 2 * tkb + 1) * 64 + lane) * 16, (u32x4){t8 | (t9 << 16), t10 | (t11 << 16), t12 | (t13 << 16), t14 | (t15 << 16)});
    }
  }
  epilogue(2, acc0);
#undef MM
  if (w < 4) {
    float s = 0.f;
#pragma unroll
    for (int i = 0; i < 4; ++i) {
      s += bfr(lo2f(gxv[i]) * lo2f(ggv[i])) + bfr(hi2f(gxv[i]) * hi2f(ggv[i]));
      s += bfr(lo2f(gxv2[i]) * lo2f(ggv2[i])) + bfr(hi2f(gxv2[i]) * hi2f(ggv2[i]));
    }
#pragma unroll
    for (int o = 16; o >= 1; o >>= 1) s += __shfl_xor(s, o);
    s = bfr(s);
    float g = bfr(1.f / (1.f + __expf(-s)));
    if (r == 0) ((float*)(ws + WS_G))[grow + h] = g;
  }

}

__global__ __launch_bounds__(512, 2) void k1_kernel(const u16* __restrict__ x, const u16* __restrict__ wqkv,
                                                    const u16* __restrict__ wo, const u16* __restrict__ w1,
                                                    const u16* __restrict__ b1, const u16* __restrict__ w2,
                                                    const u16* __restrict__ gate_w, char* __restrict__ ws) {
  __shared__ __attribute__((aligned(1024))) char lds[163840];
  const int tid = threadIdx.x, lane = tid & 63;
  const int w = __builtin_amdgcn_readfirstlane(tid >> 6);
  const int b = blockIdx.x, xcd = b & 7, idx = b >> 3;
  const int mt = (xcd & 1) * 8 + (idx & 7), j = (xcd >> 1) * 4 + (idx >> 3);
  const int c = j + 16 * (w >> 2);

  if (c < 11) k1_body<0>(x, wqkv, 1536, 96 * c, c, wo, b1, w2, gate_w, ws, lds, w, lane, mt, j, b);
  else if (c < 17) k1_body<1>(x, wqkv, 1536, 1024 + 96 * (c - 11), c, wo, b1, w2, gate_w, ws, lds, w, lane, mt, j, b);
  else k1_body<2>(x, w1, FF, 96 * (c - 17), c, wo, b1, w2, gate_w, ws, lds, w, lane, mt, j, b);
}

constexpr int VPITCH = 1056;
constexpr int K2_KOFF = 64 * VPITCH;
constexpr int K2_LDS = K2_KOFF + 65536;
constexpr float MBOUND = 48.f;

__global__ __launch_bounds__(768, 3) void k2_kernel(char* __restrict__ ws) {
  __shared__ __attribute__((aligned(16))) char lds[K2_LDS];
  const int tid = threadIdx.x, lane = tid & 63;
  const int w = __builtin_amdgcn_readfirstlane(tid >> 6);
  const int b = blockIdx.x, xcd = b & 7, idx = b >> 3;
  const int r = lane & 31, h = lane >> 5;
  const int head = xcd * 4 + (idx >> 3), qc = idx & 7;
  const unsigned lds0 = (unsigned)(uintptr_t)lds;

  if (w >= 8) {
    const int fw = w - 8;
    __builtin_amdgcn_s_setprio(3);
    {
      const char* kg = ws + WS_K + (size_t)head * 65536;
      const char* vg = ws + WS_V + (size_t)head * 65536;
      const unsigned vo = lane * 16;
#define KV(t) do { glds16(kg + (t) * 1024, vo, lds0 + K2_KOFF + (t) * 1024); glds16(vg + (t) * 1024, vo, lds0 + (t) * VPITCH); } while (0)
      KV(fw);
#pragma unroll
      for (int i = 0; i < 3; ++i) KV(4 + 4 * i + fw);
      WAITV_BAR(6);
#pragma unroll
      for (int i = 0; i < 12; ++i) KV(16 + 4 * i + fw);
      WAITV_BAR(24);
      WAITV_BAR(0);
#undef KV
    }

    const int tt = (xcd * 4 + (idx >> 3)) * 2 + (fw & 1), nb = (idx & 7) * 2 + (fw >> 1);
    const bf16x8* hp = (const bf16x8*)(ws + WS_H) + (size_t)tt * NUF * 128 + lane;
    const bf16x8* wp = (const bf16x8*)(ws + WS_W2) + (size_t)nb * 86 * 64 + lane;
    f32x16 acc;
#pragma unroll
    for (int i = 0; i < 16; ++i) acc[i] = 0.f;
    constexpr int PD = 6;
    bf16x8 ha[PD][2], wa[PD][2];
#pragma unroll
    for (int i = 0; i < PD; ++i) {
      ha[i][0] = hp[(2 * i) * 64]; ha[i][1] = hp[(2 * i + 1) * 64];
      wa[i][0] = wp[(2 * i) * 64]; wa[i][1] = wp[(2 * i + 1) * 64];
    }
    for (int u0 = 0; u0 < 42; u0 += PD) {
#pragma unroll
      for (int i = 0; i < PD; ++i) {
        acc = MFMA(wa[i][0], ha[i][0], acc);
        acc = MFMA(wa[i][1], ha[i][1], acc);
        const int un = min(u0 + i + PD, NUF - 1);
        ha[i][0] = hp[(2 * un) * 64]; ha[i][1] = hp[(2 * un + 1) * 64];
        wa[i][0] = wp[(2 * un) * 64]; wa[i][1] = wp[(2 * un + 1) * 64];
      }
    }
    acc = MFMA(wa[0][0], ha[0][0], acc);
    acc = MFMA(wa[0][1], ha[0][1], acc);
    bf16x8* bo = (bf16x8*)(ws + WS_B) + (size_t)(tt * 16 + nb) * 128 + lane;
    st16(bo, pack8(acc, 0));
    st16(bo + 64, pack8(acc, 8));

    return;
  }

  const int tt = qc * 8 + w;
  const bf16x8 qf = *(const bf16x8*)(ws + WS_Q + ((size_t)(head * SEQ + tt * 32 + r) * 2 + h) * 16);
  float km = *(const float*)(ws + WS_KM + (head * 64 + lane) * 4);
  if (lane < 16) {
    const unsigned cv = (lane & 1) ? 0u : 0x3f803f80u;
    *(__attribute__((address_space(3))) u32x4*)((lds_ptr)lds + (w * 8 + (lane >> 1)) * VPITCH + 1024 + (lane & 1) * 16) = (u32x4){cv, cv, cv, cv};
  }
  float M;
  {
#pragma unroll
    for (int of = 32; of >= 1; of >>= 1) km = fmaxf(km, __shfl_xor(km, of));
    u32x4 qu = __builtin_bit_cast(u32x4, qf);
    float ss = 0.f;
#pragma unroll
    for (int i = 0; i < 4; ++i) { float lo = lo2f(qu[i]), hi = hi2f(qu[i]); ss += lo * lo + hi * hi; }
    ss = swap32_add(ss);
    M = __builtin_sqrtf(ss * km) * 1.001f + 1e-3f;
  }
  const bool fast = __all(M <= MBOUND);

  const unsigned cst = (r == 16 || r == 20) ? 1024u : 1040u;
  lds_cptr kp = (lds_cptr)lds + K2_KOFF + lane * 16;
  lds_cptr vp0 = (lds_cptr)lds + ((r < 16) ? (h * 256 + r * 16) : cst);
  lds_cptr vp1 = (lds_cptr)lds + ((r < 16) ? (512 + h * 256 + r * 16) : cst);
  asm volatile("" : "+v"(kp), "+v"(vp0), "+v"(vp1));
  f32x16 O;
#pragma unroll
  for (int i = 0; i < 16; ++i) O[i] = 0.f;
#define LGKM_BAR() asm volatile("s_waitcnt lgkmcnt(0)\n\ts_barrier" ::: "memory")
  LGKM_BAR();

  if (fast) {
    f32x16 cin;
#pragma unroll
    for (int i = 0; i < 16; ++i) cin[i] = -M;
    f32x16 sA, sB;
    bf16x8 kfA, kfB, v0A, v1A, v0B, v1B;
    kfA = lds_b128(kp);
    kfB = lds_b128(kp + 1024);
    v0A = lds_b128(vp0); v1A = lds_b128(vp1);
    sA = MFMA(kfA, qf, cin);
#define EX2(S, o) do { S[o] = __builtin_amdgcn_exp2f(S[o]); S[o + 1] = __builtin_amdgcn_exp2f(S[o + 1]); } while (0)
#define EX4(S, o) do { EX2(S, o); EX2(S, o + 2); } while (0)
#define ATT_STEP(SC, SN, KN, KL, V0C, V1C, V0N, V1N, T, L1, L2)                     \
  do {                                                                              \
    if (!(L2)) KL = lds_b128(kp + ((T) + 2) * 1024);                                \
    if (!(L1)) { V0N = lds_b128(vp0 + ((T) + 1) * VPITCH); V1N = lds_b128(vp1 + ((T) + 1) * VPITCH); } \
    EX4(SC, 0); EX2(SC, 4); SB();                                                   \
    if (!(L1)) SN = MFMA(KN, qf, cin);                                              \
    SB(); EX2(SC, 6); EX2(SC, 8);                                                   \
    bf16x8 p0 = pack8(SC, 0); SB();                                                 \
    O = MFMA(V0C, p0, O); SB();                                                     \
    EX2(SC, 10); EX4(SC, 12);                                                       \
    bf16x8 p1 = pack8(SC, 8); SB();                                                 \
    O = MFMA(V1C, p1, O); SB();                                                     \
  } while (0)
#define ATT2(T) do { ATT_STEP(sA, sB, kfB, kfA, v0A, v1A, v0B, v1B, (T), false, false); ATT_STEP(sB, sA, kfA, kfB, v0B, v1B, v0A, v1A, (T) + 1, false, false); } while (0)
    ATT2(0);
    BAR();
    ATT2(2); ATT2(4); ATT2(6); ATT2(8); ATT2(10); ATT2(12);
    BAR();

    kp += 14 * 1024; vp0 += 14 * VPITCH; vp1 += 14 * VPITCH;
#pragma unroll 1
    for (int it = 0; it < 6; ++it) {
      ATT2(0); ATT2(2); ATT2(4); ATT2(6);
      kp += 8 * 1024; vp0 += 8 * VPITCH; vp1 += 8 * VPITCH;
    }
    ATT_STEP(sA, sB, kfB, kfA, v0A, v1A, v0B, v1B, 0, false, true);
    ATT_STEP(sB, sA, kfA, kfB, v0B, v1B, v0A, v1A, 1, true, true);
#undef ATT2
#undef ATT_STEP
  } else {
    f32x16 zero;
#pragma unroll
    for (int i = 0; i < 16; ++i) zero[i] = 0.f;
    float m = -INFINITY;
    for (int t = 0; t < 64; ++t) {
      if (t == 4 || t == 16) BAR();
      f32x16 s = MFMA(lds_b128(kp + t * 1024), qf, zero);
      bf16x8 v0 = lds_b128(vp0 + t * VPITCH), v1 = lds_b128(vp1 + t * VPITCH);
      float mx = s[0];
#pragma unroll
      for (int i = 1; i < 16; ++i) mx = fmaxf(mx, s[i]);
      mx = swap32_max(mx);
      const float mn = fmaxf(m, mx);
      const float alpha = __builtin_amdgcn_exp2f(m - mn);
      m = mn;
#pragma unroll
      for (int i = 0; i < 16; ++i) { s[i] = __builtin_amdgcn_exp2f(s[i] - mn); O[i] *= alpha; }
      O = MFMA(v0, pack8(s, 0), O);
      O = MFMA(v1, pack8(s, 8), O);
    }
  }
  {

    const float inv = 1.f / O[8];
    u32x4 o;
#pragma unroll
    for (int i = 0; i < 4; ++i) o[i] = cvtpk(O[2 * i] * inv, O[2 * i + 1] * inv);
    st16(ws + WS_O + ((size_t)(tt * 32 + head) * 64 + lane) * 16, o);
  }
}

DI void glds16x4(const void* sbase, unsigned voff, unsigned lds_base) {
  unsigned sv;
  asm volatile("s_mov_b32 %0, m0\n\ts_mov_b32 m0, %3\n\ts_nop 0\n\tglobal_load_lds_dwordx4 %1, %2\n\t"
               "global_load_lds_dwordx4 %1, %2 offset:1024\n\tglobal_load_lds_dwordx4 %1, %2 offset:2048\n\t"
               "global_load_lds_dwordx4 %1, %2 offset:3072\n\ts_mov_b32 m0, %0"
               : "=&s"(sv) : "v"(voff), "s"(sbase), "s"(lds_base) : "memory");
}
__global__ __launch_bounds__(512, 2) void k3_kernel(const char* __restrict__ ws, u16* __restrict__ out) {
  __shared__ __attribute__((aligned(16))) char lds[131072];
  const int tid = threadIdx.x, lane = tid & 63;
  const int w = __builtin_amdgcn_readfirstlane(tid >> 6);
  const int b = blockIdx.x, xcd = b & 7, idx = b >> 3;
  const int tp = xcd * 4 + (idx >> 3), np = idx & 7;
  const int r = lane & 31, h = lane >> 5;
  const unsigned lds0 = (unsigned)(uintptr_t)lds;
  const int nb = np * 2 + (w & 1), tt = tp * 2 + ((w >> 1) & 1);

  u32x4 bv0, bv1;
  float g;
  if (w < 4) {
    const char* bp = ws + WS_B + ((size_t)(tt * 16 + nb) * 128 + lane) * 16;
    const char* gp = ws + WS_G + (tt * 32 + r) * 4;
    asm volatile("global_load_dwordx4 %0, %1, off" : "=v"(bv0) : "v"(bp) : "memory");
    asm volatile("global_load_dwordx4 %0, %1, off offset:1024" : "=v"(bv1) : "v"(bp) : "memory");
    asm volatile("global_load_dword %0, %1, off" : "=v"(g) : "v"(gp) : "memory");
  }
  if (w >= 4) {
    const int ob = w & 3;
    const char* src = (ob < 2) ? ws + WS_WO + (size_t)(np * 2 + ob) * 32768 : ws + WS_O + (size_t)(tp * 2 + ob - 2) * 32768;
    const unsigned vo = lane * 16, dst = lds0 + ob * 32768;
#pragma unroll
    for (int c = 0; c < 4; ++c) glds16x4(src + c * 4096, vo, dst + c * 4096);
    WAITV_BAR(8);
#pragma unroll
    for (int c = 4; c < 8; ++c) glds16x4(src + c * 4096, vo, dst + c * 4096);
    WAITV_BAR(16); WAITV_BAR(8); WAITV_BAR(0);
    return;
  }
  lds_cptr wp = (lds_cptr)lds + (w & 1) * 32768 + lane * 16;
  lds_cptr op = (lds_cptr)lds + 65536 + (w >> 1) * 32768 + lane * 16;
  f32x16 acc;
#pragma unroll
  for (int i = 0; i < 16; ++i) acc[i] = 0.f;
  BAR();

#pragma unroll
  for (int i = 0; i < 8; ++i) acc = MFMA(lds_b128(wp + i * 1024), lds_b128(op + i * 1024), acc);
  BAR();
#pragma unroll
  for (int i = 8; i < 16; ++i) acc = MFMA(lds_b128(wp + i * 1024), lds_b128(op + i * 1024), acc);
  BAR();
#pragma unroll
  for (int i = 16; i < 24; ++i) acc = MFMA(lds_b128(wp + i * 1024), lds_b128(op + i * 1024), acc);
  asm volatile("s_waitcnt vmcnt(0)\n\ts_barrier" : "+v"(bv0), "+v"(bv1), "+v"(g)::"memory");
#pragma unroll
  for (int i = 24; i < 32; ++i) acc = MFMA(lds_b128(wp + i * 1024), lds_b128(op + i * 1024), acc);

  const float og = bfr(1.f - g);
  unsigned o2[8];
#pragma unroll
  for (int q = 0; q < 4; ++q) {
    float res[4];
#pragma unroll
    for (int i = 0; i < 4; ++i) {
      const int reg = 4 * q + i;
      const unsigned bw = (reg < 8) ? bv0[reg >> 1] : bv1[(reg - 8) >> 1];
      const float bb = (reg & 1) ? hi2f(bw) : lo2f(bw);
      res[i] = bfr(g * bfr(acc[reg])) + bfr(og * bb);
    }
    o2[2 * q] = cvtpk(res[0], res[1]);
    o2[2 * q + 1] = cvtpk(res[2], res[3]);
  }
  u16* orow = out + (size_t)(tt * 32 + r) * DM + nb * 32 + 8 * h;
#pragma unroll
  for (int k = 0; k < 2; ++k) {
    auto r0 = __builtin_amdgcn_permlane32_swap(o2[4 * k], o2[4 * k + 2], false, false);
    auto r1 = __builtin_amdgcn_permlane32_swap(o2[4 * k + 1], o2[4 * k + 3], false, false);
    *(u32x4*)(orow + 16 * k) = (u32x4){r0[0], r1[0], r0[1], r1[1]};
  }

}

extern "C" void kernel_launch(void* const* d_in, const int* in_sizes, int n_in, void* d_out, int out_size, void* d_ws,
                              size_t ws_size, hipStream_t stream) {
  const u16* x = (const u16*)d_in[0];
  const u16* wqkv = (const u16*)d_in[1];
  const u16* wo = (const u16*)d_in[2];
  const u16* w1 = (const u16*)d_in[4];
  const u16* b1 = (const u16*)d_in[5];
  const u16* w2 = (const u16*)d_in[6];
  const u16* gw = (const u16*)d_in[7];
  char* ws = (char*)d_ws;
  k1_kernel<<<256, 512, 0, stream>>>(x, wqkv, wo, w1, b1, w2, gw, ws);
  k2_kernel<<<256, 768, 0, stream>>>(ws);
  k3_kernel<<<256, 512, 0, stream>>>(ws, (u16*)d_out);
}
```
